```python
import torch

def _softmax(x, axis=-1):
    return torch.softmax(x, dim=axis)

def _gelu(x):
    return torch.nn.functional.gelu(x, approximate='tanh')

def _silu(x):
    return torch.nn.functional.silu(x)

def _sigmoid(x):
    return torch.sigmoid(x)

def _layernorm(x, axis=-1, eps=1e-05):
    m = x.mean(dim=axis, keepdim=True)
    v = x.var(dim=axis, keepdim=True, unbiased=False)
    return (x - m) / torch.sqrt(v + eps)

def _scatter_add(tgt, idx, src, axis=0):
    return tgt.clone().index_add_(axis, idx, src)

def _segment_sum(src, seg, n):
    out = torch.zeros((n,) + src.shape[1:], dtype=src.dtype)
    return out.index_add_(0, seg, src)

def _pad(x, pw):
    flat = tuple((v for pair in reversed(pw) for v in pair))
    return torch.nn.functional.pad(x, flat)
kernel_name = 'adaLN_cond__gqa_block_cached__geglu_ffn__decode'

def setup_inputs():

    _w = lambda a: (lambda t: t.to(torch.bfloat16) if t.is_floating_point() else t)(torch.as_tensor(a))

    def _A_setup():
        B, Sq, D = (8, 1, 576)
        Hq, Hkv, Sk, Dh = (32, 16, 8192, 128)
        assert Hq % Hkv == 0, (Hq, Hkv)
        return {'x': _w((lambda *s: torch.randn(*s, dtype=torch.bfloat16) * 0.1)(B, Sq, D)), 'k_cache': _w((lambda *s: torch.randn(*s, dtype=torch.bfloat16) * 0.8)(B, Hkv, Sk, Dh)), 'v_cache': _w((lambda *s: torch.randn(*s, dtype=torch.bfloat16) * 0.8)(B, Hkv, Sk, Dh)), 'wq': _w((lambda *s: torch.randn(*s, dtype=torch.bfloat16) * 0.8)(D, Hq * Dh) * D ** (-0.5)), 'wo': _w((lambda *s: torch.randn(*s, dtype=torch.bfloat16) * 0.8)(Hq * Dh, D) * (Hq * Dh) ** (-0.5))}
    _A_in = _A_setup()

    def _B_setup():
        B, S, D, F = (8, 1, 576, 4608)
        return {'x': _w((lambda *s: torch.randn(*s, dtype=torch.bfloat16) * 0.1)(B, S, D)), 'w_gate': _w((lambda *s: torch.randn(*s, dtype=torch.bfloat16) * 0.1)(D, F)), 'w_up': _w((lambda *s: torch.randn(*s, dtype=torch.bfloat16) * 0.1)(D, F)), 'w_down': _w((lambda *s: torch.randn(*s, dtype=torch.bfloat16) * 0.1)(F, D))}
    _B_in = _B_setup()
    return {'A_x': _A_in['x'], 'A_k_cache': _A_in['k_cache'], 'A_v_cache': _A_in['v_cache'], 'A_wq': _A_in['wq'], 'A_wo': _A_in['wo'], 'B_x': _B_in['x'], 'B_w_gate': _B_in['w_gate'], 'B_w_up': _B_in['w_up'], 'B_w_down': _B_in['w_down'], 'c': _w((lambda *s: torch.randn(*s, dtype=torch.bfloat16) * 0.4)(8, 576) * 8.0), 'w_mod': _w((lambda *s: torch.randn(*s, dtype=torch.bfloat16) * 0.4)(576, 6 * 576) * 576 ** (-0.5) * 8.0)}

def reference(A_x, A_k_cache, A_v_cache, A_wq, A_wo, B_x, B_w_gate, B_w_up, B_w_down, c, w_mod):

    def _A_ref(x, k_cache, v_cache, wq, wo):
        B, Sq, D = x.shape
        Hq, Dh = (32, 128)
        Hkv = k_cache.shape[1]
        rep = Hq // Hkv
        q = (lambda x, *d: x.permute(*d))((x @ wq).reshape(B, Sq, Hq, Dh), 0, 2, 1, 3)
        k = (lambda x, n, axis: torch.repeat_interleave(x, n, dim=axis))(k_cache, rep, axis=1)
        v = (lambda x, n, axis: torch.repeat_interleave(x, n, dim=axis))(v_cache, rep, axis=1)
        scores = torch.einsum('bhqd,bhkd->bhqk', q, k) * Dh ** (-0.5)
        p = _softmax(scores, axis=-1)
        o = torch.einsum('bhqk,bhkd->bhqd', p, v)
        return (lambda x, *d: x.permute(*d))(o, 0, 2, 1, 3).reshape(B, Sq, Hq * Dh) @ wo

    def _B_ref(x, w_gate, w_up, w_down):
        return _gelu(x @ w_gate) * (x @ w_up) @ w_down
    x = A_x
    mod = (_silu(c) @ w_mod).reshape(c.shape[0], 6, 1, -1)
    sa, ca, ga, sb, cb, gb = (mod[:, 0].clone(), mod[:, 1].clone(), mod[:, 2].clone(), mod[:, 3].clone(), mod[:, 4].clone(), mod[:, 5].clone())
    h = _layernorm(x, axis=-1) * (1 + ca) + sa
    x = x + ga * _A_ref(h, A_k_cache, A_v_cache, A_wq, A_wo)
    h = _layernorm(x, axis=-1) * (1 + cb) + sb
    x = x + gb * _B_ref(h, B_w_gate, B_w_up, B_w_down)
    return x
```

```cpp
#include <hip/hip_runtime.h>
#include <hip/hip_bf16.h>
#include <stdint.h>

constexpr int NB = 8, DM = 576, HQ = 32, HKV = 16, SK = 8192, DH = 128, FF = 4608;
constexpr int NSPLIT = 4;

typedef unsigned short u16;
using u32x4 = __attribute__((ext_vector_type(4))) unsigned;
using f32x4 = __attribute__((ext_vector_type(4))) float;
using bf16x2 = __attribute__((ext_vector_type(2))) __bf16;

constexpr size_t WS_CNT = 0;
constexpr size_t WS_FLG = 4096;
constexpr size_t WS_MOD = 8192;
constexpr size_t WS_Q = WS_MOD + (size_t)NB * 6 * DM * 4;
constexpr size_t WS_PML = WS_Q + NB * HQ * DH * 2;
constexpr size_t WS_PO = WS_PML + 65536;
constexpr size_t WS_P4 = WS_PO + (size_t)NB * HQ * 16 * DH * 4;
constexpr size_t WS_X2 = WS_P4 + (size_t)32 * NB * DM * 4;
constexpr size_t WS_GU = WS_X2 + 32768;
constexpr size_t WS_P6 = WS_GU + (size_t)2 * NB * FF * 4;

__device__ __forceinline__ float bf2f(u16 u) { return __uint_as_float(((unsigned)u) << 16); }
__device__ __forceinline__ u16 f2bf(float x) {
  __hip_bfloat16 b = __float2bfloat16(x);
  return __builtin_bit_cast(u16, b);
}
__device__ __forceinline__ float rbf(float x) { return bf2f(f2bf(x)); }
__device__ __forceinline__ float lo16(unsigned w) { return __uint_as_float(w << 16); }
__device__ __forceinline__ float hi16(unsigned w) { return __uint_as_float(w & 0xffff0000u); }

template <int CTRL> __device__ __forceinline__ float dpp(float x) {
  return __builtin_bit_cast(float, __builtin_amdgcn_mov_dpp(__builtin_bit_cast(int, x), CTRL, 0xf, 0xf, true));
}
constexpr int XOR1 = 0xB1, XOR2 = 0x4E, XOR7 = 0x141, XOR8 = 0x128, ROR4 = 0x124;
__device__ __forceinline__ float xrow16_max(float x) {
  auto s = __builtin_amdgcn_permlane16_swap(__float_as_uint(x), __float_as_uint(x), false, false);
  x = fmaxf(__uint_as_float(s[0]), __uint_as_float(s[1]));
  auto t = __builtin_amdgcn_permlane32_swap(__float_as_uint(x), __float_as_uint(x), false, false);
  return fmaxf(__uint_as_float(t[0]), __uint_as_float(t[1]));
}
__device__ __forceinline__ float xrow16_sum(float x) {
  auto s = __builtin_amdgcn_permlane16_swap(__float_as_uint(x), __float_as_uint(x), false, false);
  x = __uint_as_float(s[0]) + __uint_as_float(s[1]);
  auto t = __builtin_amdgcn_permlane32_swap(__float_as_uint(x), __float_as_uint(x), false, false);
  return __uint_as_float(t[0]) + __uint_as_float(t[1]);
}

using bf16x8 = __attribute__((ext_vector_type(8))) __bf16;
__device__ __forceinline__ void wtile_load(const u16* __restrict__ W, int ldw, int k0, int n0, int ncols, u32x4 (&w)[8]) {
  const int lane = threadIdx.x & 63, g = lane >> 4, c = lane & 15;
  int col = n0 + 8 * c;
  col = col < ncols ? col : 0;
  const u16* p = W + (size_t)(k0 + g * 8) * ldw + col;
#pragma unroll
  for (int i = 0; i < 8; ++i) w[i] = *reinterpret_cast<const u32x4*>(p + (size_t)i * ldw);
}
__device__ __forceinline__ void wtile_mfma(const u32x4 (&w)[8], const u16* sAct, int lda, int k0, f32x4 (&acc)[8]) {
  const int lane = threadIdx.x & 63, g = lane >> 4, c = lane & 15;
  const u32x4 af = *reinterpret_cast<const u32x4*>(sAct + (c & 7) * lda + k0 + g * 8);
#pragma unroll
  for (int j = 0; j < 8; ++j) {
    u32x4 bf;
#pragma unroll
    for (int i = 0; i < 4; ++i)
      bf[i] = __builtin_amdgcn_perm(w[2 * i + 1][j >> 1], w[2 * i][j >> 1], (j & 1) ? 0x07060302u : 0x05040100u);
    acc[j] = __builtin_amdgcn_mfma_f32_16x16x32_bf16(__builtin_bit_cast(bf16x8, af), __builtin_bit_cast(bf16x8, bf), acc[j], 0, 0, 0);
  }
}
__device__ __forceinline__ void wtile_store(const f32x4 (&acc)[8], float* red, int wave) {
  const int lane = threadIdx.x & 63, g = lane >> 4, c = lane & 15;
  if (g < 2) {
#pragma unroll
    for (int i = 0; i < 4; ++i) {
      float* p = red + wave * 1024 + (g * 4 + i) * 128 + 8 * c;
      *reinterpret_cast<f32x4*>(p) = f32x4{acc[0][i], acc[1][i], acc[2][i], acc[3][i]};
      *reinterpret_cast<f32x4*>(p + 4) = f32x4{acc[4][i], acc[5][i], acc[6][i], acc[7][i]};
    }
  }
}

constexpr int LDA = DM + 8;

__device__ __forceinline__ float wave_sum(float v) {
  v += dpp<XOR1>(v);
  v += dpp<XOR2>(v);
  v += dpp<ROR4>(v);
  v += dpp<XOR8>(v);
  return xrow16_sum(v);
}
__device__ __forceinline__ void adaln_row(const float (&xv)[9], const float (&sh)[9], const float (&sc)[9], u16* sRow) {
  const int lane = threadIdx.x & 63;
  float s = 0.f;
#pragma unroll
  for (int i = 0; i < 9; ++i) s += xv[i];
  const float mean = wave_sum(s) * (1.f / DM);
  float q = 0.f;
#pragma unroll
  for (int i = 0; i < 9; ++i) { const float d = xv[i] - mean; q = fmaf(d, d, q); }
  const float var = rbf(wave_sum(q) * (1.f / DM));
  const float den = rbf(sqrtf(rbf(var + 1e-5f)));
  const float mb = rbf(mean);
#pragma unroll
  for (int i = 0; i < 9; ++i) {
    const float ln = rbf(rbf(xv[i] - mb) / den);
    sRow[lane + 64 * i] = f2bf(rbf(ln * rbf(1.f + sc[i])) + sh[i]);
  }
}

__global__ void __launch_bounds__(576) k_mod(const u16* __restrict__ c, const u16* __restrict__ wmod, unsigned char* __restrict__ ws) {
  __shared__ __attribute__((aligned(16))) u16 sAct[NB * LDA];
  __shared__ __attribute__((aligned(16))) float red[9 * 1024];
  const int tid = threadIdx.x, wave = tid >> 6, n0 = blockIdx.x * 128;
  if (blockIdx.x == 0 && tid < 512) reinterpret_cast<u32x4*>(ws + WS_CNT)[tid] = u32x4{0u, 0u, 0u, 0u};
  u32x4 w0[8], w1[8];
  wtile_load(wmod, 6 * DM, wave * 64, n0, 6 * DM, w0);
  wtile_load(wmod, 6 * DM, wave * 64 + 32, n0, 6 * DM, w1);
  {
    const u32x4 cv = reinterpret_cast<const u32x4*>(c)[tid];
    const int b = tid / 72, k = (tid % 72) * 8;
    u32x4 o;
#pragma unroll
    for (int j = 0; j < 4; ++j) {
      const float c0 = lo16(cv[j]), c1 = hi16(cv[j]);
      o[j] = (unsigned)f2bf(c0 / (1.f + __expf(-c0))) | ((unsigned)f2bf(c1 / (1.f + __expf(-c1))) << 16);
    }
    *reinterpret_cast<u32x4*>(sAct + b * LDA + k) = o;
  }
  __syncthreads();
  f32x4 acc[8];
#pragma unroll
  for (int j = 0; j < 8; ++j) acc[j] = f32x4{0.f, 0.f, 0.f, 0.f};
  wtile_mfma(w0, sAct, LDA, wave * 64, acc);
  wtile_mfma(w1, sAct, LDA, wave * 64 + 32, acc);
  wtile_store(acc, red, wave);
  __syncthreads();
  float* mod = reinterpret_cast<float*>(ws + WS_MOD);
  for (int o = tid; o < 1024; o += 576) {
    float s = 0.f;
#pragma unroll
    for (int w = 0; w < 9; ++w) s += red[w * 1024 + o];
    mod[(o >> 7) * (6 * DM) + n0 + (o & 127)] = rbf(s);
  }
}

__device__ __forceinline__ float dot2(unsigned k, unsigned q, float acc) {
  return __builtin_amdgcn_fdot2_f32_bf16(__builtin_bit_cast(bf16x2, k), __builtin_bit_cast(bf16x2, q), acc, false);
}
constexpr int AW = 4;
constexpr int PAGE = 32;
constexpr int KPS = SK / NSPLIT;
constexpr int NPW = KPS / PAGE / AW;

typedef __attribute__((address_space(3))) void* lds_ptr_t;
__global__ void __launch_bounds__(AW * 64, 2) k_attn(const u16* __restrict__ x, const u16* __restrict__ wq,
                                                     const u16* __restrict__ kc, const u16* __restrict__ vc,
                                                     unsigned char* __restrict__ ws) {
  __shared__ __attribute__((aligned(16))) unsigned char sKV[AW * 16384];
  __shared__ __attribute__((aligned(16))) float sP[AW * 64];
  __shared__ __attribute__((aligned(16))) float sM[AW * 2 * (4 + DH)];
  const int s = blockIdx.x, kvh = blockIdx.y, b = blockIdx.z;
  const int wg = (b * HKV + kvh) * NSPLIT + s;
  const int tid = threadIdx.x, lane = tid & 63, wave = __builtin_amdgcn_readfirstlane(tid >> 6), r = lane >> 4, c = lane & 15;
  const size_t base = ((size_t)(b * HKV + kvh) * SK + (size_t)s * KPS + (size_t)wave * PAGE + r) * DH + 8 * c;
  const u32x4* kp = reinterpret_cast<const u32x4*>(kc + base);
  const u32x4* vp = reinterpret_cast<const u32x4*>(vc + base);
  constexpr int TSTR = 4 * DH / 8;
  constexpr int PSTR = AW * PAGE * DH / 8;
  unsigned* flags = reinterpret_cast<unsigned*>(ws + WS_FLG);
  u16* qws = reinterpret_cast<u16*>(ws + WS_Q);

  if (wg < HQ) {
    u16* sAct = reinterpret_cast<u16*>(sKV);
    float* red = reinterpret_cast<float*>(sKV + 12288);
    const int n0 = wg * 128;
    u32x4 w[5][8];
#pragma unroll
    for (int i = 0; i < 5; ++i) {
      const int st = wave + 4 * i;
      wtile_load(wq, HQ * DH, (st < 18 ? st : 0) * 32, n0, HQ * DH, w[i]);
    }
#pragma unroll
    for (int rr = 0; rr < 2; ++rr) {
      const int row = wave * 2 + rr;
      const float* mod = reinterpret_cast<const float*>(ws + WS_MOD) + row * 6 * DM + lane;
      float xv[9], sh[9], sc[9];
#pragma unroll
      for (int i = 0; i < 9; ++i) { xv[i] = bf2f(x[row * DM + lane + 64 * i]); sh[i] = mod[64 * i]; sc[i] = mod[DM + 64 * i]; }
      adaln_row(xv, sh, sc, sAct + row * LDA);
    }
    __syncthreads();
    f32x4 acc[8];
#pragma unroll
    for (int j = 0; j < 8; ++j) acc[j] = f32x4{0.f, 0.f, 0.f, 0.f};
#pragma unroll
    for (int i = 0; i < 5; ++i) {
      const int st = wave + 4 * i;
      if (st < 18) wtile_mfma(w[i], sAct, LDA, st * 32, acc);
    }
    wtile_store(acc, red, wave);
    __syncthreads();
    {
      const int o = tid * 4;
      float sv[4];
#pragma unroll
      for (int k = 0; k < 4; ++k) sv[k] = red[o + k] + red[1024 + o + k] + red[2048 + o + k] + red[3072 + o + k];
      const unsigned long long pk = (unsigned long long)((unsigned)f2bf(sv[0]) | ((unsigned)f2bf(sv[1]) << 16)) |
                                    ((unsigned long long)((unsigned)f2bf(sv[2]) | ((unsigned)f2bf(sv[3]) << 16)) << 32);
      __hip_atomic_store(reinterpret_cast<unsigned long long*>(qws + (o >> 7) * (HQ * DH) + n0 + (o & 127)), pk,
                         __ATOMIC_RELAXED, __HIP_MEMORY_SCOPE_AGENT);
    }
    asm volatile("s_waitcnt vmcnt(0)" ::: "memory");
    __syncthreads();
    if (tid == 0) __hip_atomic_store(flags + wg * 16, 1u, __ATOMIC_RELAXED, __HIP_MEMORY_SCOPE_AGENT);
  }
  unsigned char* sW = sKV + wave * 16384;
#pragma unroll
  for (int t = 0; t < 8; ++t) {
    __builtin_amdgcn_global_load_lds(kp + t * TSTR, (lds_ptr_t)(sW + t * 1024), 16, 0, 2);
    __builtin_amdgcn_global_load_lds(vp + t * TSTR, (lds_ptr_t)(sW + 8192 + t * 1024), 16, 0, 2);
  }
  u32x4 Kn[8], Vn[8];
#pragma unroll
  for (int t = 0; t < 8; ++t) Kn[t] = __builtin_nontemporal_load(kp + PSTR + t * TSTR);
#pragma unroll
  for (int t = 0; t < 8; ++t) Vn[t] = __builtin_nontemporal_load(vp + PSTR + t * TSTR);
  if (wave == 0) {
    for (int spin = 0; spin < (1 << 20); ++spin) {
      const unsigned f0 = __hip_atomic_load(flags + (kvh * 2) * 16, __ATOMIC_RELAXED, __HIP_MEMORY_SCOPE_AGENT);
      const unsigned f1 = __hip_atomic_load(flags + (kvh * 2 + 1) * 16, __ATOMIC_RELAXED, __HIP_MEMORY_SCOPE_AGENT);
      if ((f0 & f1) != 0u) break;
      __builtin_amdgcn_s_sleep(2);
    }
  }
  __syncthreads();
  u32x4 q0, q1;
  {
    unsigned long long* qg = reinterpret_cast<unsigned long long*>(qws + b * (HQ * DH) + kvh * 2 * DH + 8 * c);
    const unsigned long long a0 = __hip_atomic_load(qg, __ATOMIC_RELAXED, __HIP_MEMORY_SCOPE_AGENT);
    const unsigned long long a1 = __hip_atomic_load(qg + 1, __ATOMIC_RELAXED, __HIP_MEMORY_SCOPE_AGENT);
    const unsigned long long b0 = __hip_atomic_load(qg + 32, __ATOMIC_RELAXED, __HIP_MEMORY_SCOPE_AGENT);
    const unsigned long long b1 = __hip_atomic_load(qg + 33, __ATOMIC_RELAXED, __HIP_MEMORY_SCOPE_AGENT);
    q0 = u32x4{(unsigned)a0, (unsigned)(a0 >> 32), (unsigned)a1, (unsigned)(a1 >> 32)};
    q1 = u32x4{(unsigned)b0, (unsigned)(b0 >> 32), (unsigned)b1, (unsigned)(b1 >> 32)};
  }
  float m = -INFINITY, l = 0.f;
  float acc[16];
#pragma unroll
  for (int k = 0; k < 16; ++k) acc[k] = 0.f;
  constexpr float SC = 0.088388347648318440f;
  constexpr float L2E = 1.4426950408889634f;
  float* prow = sP + (wave * 4 + r) * 16;
  asm volatile("s_waitcnt vmcnt(0)" ::: "memory");
  for (int i = 0; i < NPW; ++i) {
    u32x4 Kc[8], Vc[8];
    if (i == 0) {
#pragma unroll
      for (int t = 0; t < 8; ++t) {
        Kc[t] = *reinterpret_cast<const u32x4*>(sW + t * 1024 + lane * 16);
        Vc[t] = *reinterpret_cast<const u32x4*>(sW + 8192 + t * 1024 + lane * 16);
      }
    } else {
#pragma unroll
      for (int t = 0; t < 8; ++t) { Kc[t] = Kn[t]; Vc[t] = Vn[t]; }
      if (i + 1 < NPW) {
        const u32x4* kq = kp + (size_t)(i + 1) * PSTR;
        const u32x4* vq = vp + (size_t)(i + 1) * PSTR;
#pragma unroll
        for (int t = 0; t < 8; ++t) Kn[t] = __builtin_nontemporal_load(kq + t * TSTR);
#pragma unroll
        for (int t = 0; t < 8; ++t) Vn[t] = __builtin_nontemporal_load(vq + t * TSTR);
      }
    }
    float x[16];
#pragma unroll
    for (int t = 0; t < 8; ++t) {
      float d0 = 0.f, d1 = 0.f;
#pragma unroll
      for (int k = 0; k < 4; ++k) { d0 = dot2(Kc[t][k], q0[k], d0); d1 = dot2(Kc[t][k], q1[k], d1); }
      x[2 * t] = d0; x[2 * t + 1] = d1;
    }
#define TR_STEP(HALF, CTRL, BIT) _Pragma("unroll") for (int h = 0; h < (HALF); ++h) { \
      const float lo_ = x[h], hi_ = x[h + (HALF)]; const float t1_ = lo_ + dpp<CTRL>(lo_), t2_ = hi_ + dpp<CTRL>(hi_); \
      x[h] = (c & (BIT)) ? t2_ : t1_; }
    TR_STEP(8, XOR8, 8) TR_STEP(4, XOR7, 4) TR_STEP(2, XOR2, 2) TR_STEP(1, XOR1, 1)
#undef TR_STEP
    const float sc = rbf(rbf(x[0]) * SC) * L2E;
    float pm = sc;
    pm = fmaxf(pm, dpp<XOR2>(pm)); pm = fmaxf(pm, dpp<ROR4>(pm)); pm = fmaxf(pm, dpp<XOR8>(pm)); pm = xrow16_max(pm);
    const float mn = fmaxf(m, pm);
    const float alpha = __builtin_amdgcn_exp2f(m - mn);
    const float p = __builtin_amdgcn_exp2f(sc - mn);
    l = fmaf(l, alpha, p); m = mn;
    const float a0 = dpp<0xA0>(alpha);
    const float a1 = dpp<0xF5>(alpha);
#pragma unroll
    for (int k = 0; k < 8; ++k) { acc[k] *= a0; acc[8 + k] *= a1; }
    prow[c] = p;
    asm volatile("s_waitcnt lgkmcnt(0)" ::: "memory");
    f32x4 pr[4];
#pragma unroll
    for (int u = 0; u < 4; ++u) pr[u] = *reinterpret_cast<const f32x4*>(prow + 4 * u);
#pragma unroll
    for (int t = 0; t < 8; ++t) {
      const float p0 = pr[t >> 1][(t & 1) * 2], p1 = pr[t >> 1][(t & 1) * 2 + 1];
#pragma unroll
      for (int k = 0; k < 4; ++k) {
        const float vl = lo16(Vc[t][k]), vh = hi16(Vc[t][k]);
        acc[2 * k] = fmaf(p0, vl, acc[2 * k]);
        acc[2 * k + 1] = fmaf(p0, vh, acc[2 * k + 1]);
        acc[8 + 2 * k] = fmaf(p1, vl, acc[8 + 2 * k]);
        acc[8 + 2 * k + 1] = fmaf(p1, vh, acc[8 + 2 * k + 1]);
      }
    }
  }
#pragma unroll
  for (int k = 0; k < 16; ++k) acc[k] = xrow16_sum(acc[k]);
  {
    float t = l; t += dpp<XOR2>(t); t += dpp<ROR4>(t); t += dpp<XOR8>(t); l = xrow16_sum(t);
  }
  if (r == 0) {
#pragma unroll
    for (int g = 0; g < 2; ++g) {
      float* rec = sM + (wave * 2 + g) * (4 + DH);
      *reinterpret_cast<f32x4*>(rec + 4 + 8 * c) = f32x4{acc[g * 8], acc[g * 8 + 1], acc[g * 8 + 2], acc[g * 8 + 3]};
      *reinterpret_cast<f32x4*>(rec + 4 + 8 * c + 4) = f32x4{acc[g * 8 + 4], acc[g * 8 + 5], acc[g * 8 + 6], acc[g * 8 + 7]};
      if (c == g) { rec[0] = m; rec[1] = l; }
    }
  }
  __syncthreads();
  if (wave < 2) {
    const int g = wave, d0 = 2 * lane;
    float M = -INFINITY;
#pragma unroll
    for (int w = 0; w < AW; ++w) M = fmaxf(M, sM[(w * 2 + g) * (4 + DH)]);
    float L = 0.f, o0 = 0.f, o1 = 0.f;
#pragma unroll
    for (int w = 0; w < AW; ++w) {
      const float* rec = sM + (w * 2 + g) * (4 + DH);
      const float wt = __builtin_amdgcn_exp2f(rec[0] - M);
      L = fmaf(wt, rec[1], L); o0 = fmaf(wt, rec[4 + d0], o0); o1 = fmaf(wt, rec[4 + d0 + 1], o1);
    }
    const size_t slot = ((size_t)(b * HQ + kvh * 2 + g)) * NSPLIT + s;
    float* po = reinterpret_cast<float*>(ws + WS_PO) + slot * DH + d0;
    *reinterpret_cast<float2*>(po) = make_float2(o0, o1);
    if (lane == 0) *reinterpret_cast<float2*>(reinterpret_cast<float*>(ws + WS_PML) + slot * 2) = make_float2(M, L);
  }
}

__device__ __forceinline__ void st_wt(float* p, float a, float b) {
  __hip_atomic_store(reinterpret_cast<unsigned long long*>(p), __builtin_bit_cast(unsigned long long, make_float2(a, b)),
                     __ATOMIC_RELAXED, __HIP_MEMORY_SCOPE_AGENT);
}
__device__ __forceinline__ float gelu_tanh(float x) {
  const float u = 0.7978845608028654f * (x + 0.044715f * x * x * x);
  return x / (1.f + __expf(-2.f * u));
}
__device__ __forceinline__ float ld_sc1(const float* p) {
  return __builtin_bit_cast(float, __hip_atomic_load(reinterpret_cast<const unsigned*>(p), __ATOMIC_RELAXED, __HIP_MEMORY_SCOPE_AGENT));
}
__device__ __forceinline__ float2 ld2_sc1(const float* p) {
  return __builtin_bit_cast(float2, __hip_atomic_load(reinterpret_cast<const unsigned long long*>(p), __ATOMIC_RELAXED, __HIP_MEMORY_SCOPE_AGENT));
}
__device__ __forceinline__ void wait_count(unsigned* cnt, unsigned n) {
  if (threadIdx.x < 64) {
    for (int spin = 0; spin < (1 << 20); ++spin) {
      if (__hip_atomic_load(cnt, __ATOMIC_RELAXED, __HIP_MEMORY_SCOPE_AGENT) >= n) break;
      __builtin_amdgcn_s_sleep(1);
    }
  }
  __syncthreads();
}
__device__ __forceinline__ unsigned arrive(unsigned* cnt, unsigned* sFlag) {
  asm volatile("s_waitcnt vmcnt(0)" ::: "memory");
  __syncthreads();
  if (threadIdx.x == 0) *sFlag = __hip_atomic_fetch_add(cnt, 1u, __ATOMIC_RELAXED, __HIP_MEMORY_SCOPE_AGENT);
  __syncthreads();
  return *sFlag;
}

constexpr int N_C = HKV * 5, N_D = (FF / 128) * 2, N_E = (FF / 256) * 5;
constexpr int CNT_CT = 0, CNT_F = 5, CNT_X = 10, CNT_G = 11;

__global__ void __launch_bounds__(576, 3) k_tail(const u16* __restrict__ x, const u16* __restrict__ wo, const u16* __restrict__ wg,
                                              const u16* __restrict__ wu, const u16* __restrict__ wd,
                                              unsigned char* __restrict__ ws, u16* __restrict__ out) {
  __shared__ __attribute__((aligned(16))) u16 sAct[NB * LDA];
  __shared__ __attribute__((aligned(16))) float red[9 * 1024];
  __shared__ unsigned sFlag;
  const int tid = threadIdx.x, wave = tid >> 6, lane = tid & 63, blk = blockIdx.x;
  unsigned* cnt = reinterpret_cast<unsigned*>(ws + WS_CNT);
  const float* mod = reinterpret_cast<const float*>(ws + WS_MOD);
  float* x2p = reinterpret_cast<float*>(ws + WS_X2);
  float* gup = reinterpret_cast<float*>(ws + WS_GU);
  f32x4 acc[8];
#pragma unroll
  for (int j = 0; j < 8; ++j) acc[j] = f32x4{0.f, 0.f, 0.f, 0.f};

  if (blk < N_C) {
    const int kvh = blk % HKV, ct = blk / HKV, n0 = ct * 128;
    u32x4 w0[8];
    if (wave < 8) {
      wtile_load(wo, DM, kvh * 256 + wave * 32, n0, DM, w0);
      const float* pml = reinterpret_cast<const float*>(ws + WS_PML);
      const float* po = reinterpret_cast<const float*>(ws + WS_PO);
      const int b = tid >> 6, g = (tid >> 5) & 1, d = (tid & 31) * 4;
      const size_t slot = (size_t)(b * HQ + kvh * 2 + g) * NSPLIT;
      float2 ml[NSPLIT];
      f32x4 ov[NSPLIT];
#pragma unroll
      for (int s = 0; s < NSPLIT; ++s) {
        ml[s] = *reinterpret_cast<const float2*>(pml + (slot + s) * 2);
        ov[s] = *reinterpret_cast<const f32x4*>(po + (slot + s) * DH + d);
      }
      float M = -INFINITY;
#pragma unroll
      for (int s = 0; s < NSPLIT; ++s) M = fmaxf(M, ml[s].x);
      float L = 0.f;
      f32x4 o = {0.f, 0.f, 0.f, 0.f};
#pragma unroll
      for (int s = 0; s < NSPLIT; ++s) {
        const float wt = __builtin_amdgcn_exp2f(ml[s].x - M);
        L = fmaf(wt, ml[s].y, L);
        o += wt * ov[s];
      }
      const float inv = 1.f / L;
      uint2 pk;
      pk.x = (unsigned)f2bf(o[0] * inv) | ((unsigned)f2bf(o[1] * inv) << 16);
      pk.y = (unsigned)f2bf(o[2] * inv) | ((unsigned)f2bf(o[3] * inv) << 16);
      *reinterpret_cast<uint2*>(sAct + b * 264 + g * 128 + d) = pk;
    }
    __syncthreads();
    if (wave < 8) {
      wtile_mfma(w0, sAct, 264, wave * 32, acc);
      wtile_store(acc, red, wave);
    }
    __syncthreads();
    const int o = tid * 2, b = (o >> 7) & 7, col = n0 + (o & 127);
    const bool valid = tid < 512 && col < DM;
    float* p4 = reinterpret_cast<float*>(ws + WS_P4);
    if (valid) {
      float s0 = 0.f, s1 = 0.f;
#pragma unroll
      for (int w = 0; w < 8; ++w) { s0 += red[w * 1024 + o]; s1 += red[w * 1024 + o + 1]; }
      st_wt(p4 + ((size_t)kvh * NB + b) * DM + col, s0, s1);
    }
    if (arrive(cnt + (CNT_CT + ct) * 16, &sFlag) != HKV - 1) return;
    if (valid) {
      float s0 = 0.f, s1 = 0.f;
#pragma unroll
      for (int h = 0; h < HKV; ++h) {
        const float2 v = ld2_sc1(p4 + ((size_t)h * NB + b) * DM + col);
        s0 += v.x; s1 += v.y;
      }
      const float* ga = mod + b * 6 * DM + 2 * DM + col;
      st_wt(x2p + b * DM + col, rbf(bf2f(x[b * DM + col]) + rbf(ga[0] * rbf(s0))), rbf(bf2f(x[b * DM + col + 1]) + rbf(ga[1] * rbf(s1))));
    }
    arrive(cnt + CNT_X * 16, &sFlag);
    return;
  }
  if (blk < N_C + N_D) {
    const int u = blk - N_C, tile = u >> 1, mat = u & 1, n0 = tile * 128;
    const u16* W = mat ? wu : wg;
    u32x4 w0[8], w1[8];
    wtile_load(W, FF, wave * 64, n0, FF, w0);
    wtile_load(W, FF, wave * 64 + 32, n0, FF, w1);
    float sh[9], sc[9];
    if (wave < NB) {
      const float* mp = mod + wave * 6 * DM + 3 * DM + lane;
#pragma unroll
      for (int i = 0; i < 9; ++i) { sh[i] = mp[64 * i]; sc[i] = mp[DM + 64 * i]; }
    }
    wait_count(cnt + CNT_X * 16, 5);
    if (wave < NB) {
      float xv[9];
#pragma unroll
      for (int i = 0; i < 9; ++i) xv[i] = ld_sc1(x2p + wave * DM + lane + 64 * i);
      adaln_row(xv, sh, sc, sAct + wave * LDA);
    }
    __syncthreads();
    wtile_mfma(w0, sAct, LDA, wave * 64, acc);
    wtile_mfma(w1, sAct, LDA, wave * 64 + 32, acc);
    wtile_store(acc, red, wave);
    __syncthreads();
    if (tid < 512) {
      const int o = tid * 2;
      float s0 = 0.f, s1 = 0.f;
#pragma unroll
      for (int w = 0; w < 9; ++w) { s0 += red[w * 1024 + o]; s1 += red[w * 1024 + o + 1]; }
      st_wt(gup + (size_t)mat * NB * FF + (o >> 7) * FF + n0 + (o & 127), rbf(s0), rbf(s1));
    }
    arrive(cnt + (CNT_G + (tile >> 1)) * 16, &sFlag);
    return;
  }
  {
    const int u = blk - N_C - N_D, ks = u % (FF / 256), ct = u / (FF / 256), n0 = ct * 128;
    u32x4 w0[8];
    if (wave < 8) wtile_load(wd, DM, ks * 256 + wave * 32, n0, DM, w0);
    wait_count(cnt + (CNT_G + ks) * 16, 4);
    if (wave < 8) {
      const int b = tid >> 6, k = (tid & 63) * 4;
      const float* gu = gup + b * FF + ks * 256 + k;
      const float2 g0 = ld2_sc1(gu), g1 = ld2_sc1(gu + 2), u0 = ld2_sc1(gu + (size_t)NB * FF), u1 = ld2_sc1(gu + (size_t)NB * FF + 2);
      uint2 pk;
      pk.x = (unsigned)f2bf(rbf(gelu_tanh(g0.x)) * u0.x) | ((unsigned)f2bf(rbf(gelu_tanh(g0.y)) * u0.y) << 16);
      pk.y = (unsigned)f2bf(rbf(gelu_tanh(g1.x)) * u1.x) | ((unsigned)f2bf(rbf(gelu_tanh(g1.y)) * u1.y) << 16);
      *reinterpret_cast<uint2*>(sAct + b * 264 + k) = pk;
    }
    __syncthreads();
    if (wave < 8) {
      wtile_mfma(w0, sAct, 264, wave * 32, acc);
      wtile_store(acc, red, wave);
    }
    __syncthreads();
    const int o = tid * 2, b = (o >> 7) & 7, col = n0 + (o & 127);
    const bool valid = tid < 512 && col < DM;
    float* p6 = reinterpret_cast<float*>(ws + WS_P6);
    if (valid) {
      float s0 = 0.f, s1 = 0.f;
#pragma unroll
      for (int w = 0; w < 8; ++w) { s0 += red[w * 1024 + o]; s1 += red[w * 1024 + o + 1]; }
      st_wt(p6 + ((size_t)ks * NB + b) * DM + col, s0, s1);
    }
    if (arrive(cnt + (CNT_F + ct) * 16, &sFlag) != FF / 256 - 1) return;
    if (valid) {
      float s0 = 0.f, s1 = 0.f;
#pragma unroll
      for (int h = 0; h < FF / 256; ++h) {
        const float2 v = ld2_sc1(p6 + ((size_t)h * NB + b) * DM + col);
        s0 += v.x; s1 += v.y;
      }
      const float* gb = mod + b * 6 * DM + 5 * DM + col;
      const float2 xx = ld2_sc1(x2p + b * DM + col);
      *reinterpret_cast<unsigned*>(out + b * DM + col) =
          (unsigned)f2bf(xx.x + rbf(gb[0] * rbf(s0))) | ((unsigned)f2bf(xx.y + rbf(gb[1] * rbf(s1))) << 16);
    }
  }
}

extern "C" void kernel_launch(void* const* d_in, const int* in_sizes, int n_in, void* d_out, int out_size, void* d_ws,
                              size_t ws_size, hipStream_t stream) {
  const u16* x = (const u16*)d_in[0];
  const u16* kc = (const u16*)d_in[1];
  const u16* vc = (const u16*)d_in[2];
  const u16* wq = (const u16*)d_in[3];
  const u16* wo = (const u16*)d_in[4];
  const u16* wg = (const u16*)d_in[6];
  const u16* wu = (const u16*)d_in[7];
  const u16* wd = (const u16*)d_in[8];
  const u16* c = (const u16*)d_in[9];
  const u16* wmod = (const u16*)d_in[10];
  unsigned char* ws = (unsigned char*)d_ws;
  k_mod<<<27, 576, 0, stream>>>(c, wmod, ws);
  k_attn<<<dim3(NSPLIT, HKV, NB), AW * 64, 0, stream>>>(x, wq, kc, vc, ws);
  k_tail<<<N_C + N_D + N_E, 576, 0, stream>>>(x, wo, wg, wu, wd, ws, (u16*)d_out);
}
```
